```python
import torch

def _softmax(x, axis=-1):
    return torch.softmax(x, dim=axis)
def _gelu(x):
    return torch.nn.functional.gelu(x, approximate="tanh")
def _silu(x):
    return torch.nn.functional.silu(x)
def _sigmoid(x):
    return torch.sigmoid(x)
def _layernorm(x, axis=-1, eps=1e-5):
    m = x.mean(dim=axis, keepdim=True)
    v = x.var(dim=axis, keepdim=True, unbiased=False)
    return (x - m) / torch.sqrt(v + eps)
def _scatter_add(tgt, idx, src, axis=0):
    return tgt.clone().index_add_(axis, idx, src)
def _segment_sum(src, seg, n):
    out = torch.zeros((n,) + src.shape[1:], dtype=src.dtype)
    return out.index_add_(0, seg, src)
def _pad(x, pw):
    flat = tuple(v for pair in reversed(pw) for v in pair)
    return torch.nn.functional.pad(x, flat)

kernel_name = 'standalone__low_rank_proj_linformer__prompt'

def setup_inputs():

    _w = (lambda a: (lambda t: t.to(torch.bfloat16) if t.is_floating_point() else t)(torch.as_tensor(a)))
    B, H, Sq, Skv, Dh, R = 1, 32, 2048, 2048, 192, 8
    return {
        "q": _w((lambda *s: torch.randn(*s, dtype=torch.bfloat16) * 0.8)(B, H, Sq, Dh)),
        "k": _w((lambda *s: torch.randn(*s, dtype=torch.bfloat16) * 0.8)(B, H, Skv, Dh)),
        "v": _w((lambda *s: torch.randn(*s, dtype=torch.bfloat16) * 0.05)(B, H, Skv, Dh)),
        "e_proj": _w((lambda *s: torch.randn(*s, dtype=torch.bfloat16) * 0.8)(R, Skv) * (Skv ** -0.5)),
        "f_proj": _w((lambda *s: torch.randn(*s, dtype=torch.bfloat16) * 0.05)(R, Skv) * (Skv ** -0.5)),
    }

def reference(q, k, v, e_proj, f_proj):
    B, H, Sq, Dh = q.shape
    kp = torch.einsum("rn,bhnd->bhrd", e_proj, k)
    vp = torch.einsum("rn,bhnd->bhrd", f_proj, v)
    scores = torch.einsum("bhqd,bhrd->bhqr", q, kp) * (Dh ** -0.5)
    p = _softmax(scores, axis=-1)
    o = torch.einsum("bhqr,bhrd->bhqd", p, vp)
    return o
```

```cpp
#include <hip/hip_runtime.h>
#include <hip/hip_bf16.h>

typedef unsigned v4u __attribute__((ext_vector_type(4)));
typedef unsigned v2u __attribute__((ext_vector_type(2)));
typedef float f32x4 __attribute__((ext_vector_type(4)));
typedef float f32x2 __attribute__((ext_vector_type(2)));
typedef __bf16 bf8 __attribute__((ext_vector_type(8)));
typedef __bf16 bf2 __attribute__((ext_vector_type(2)));
typedef short s4 __attribute__((ext_vector_type(4)));
#define GAS __attribute__((address_space(1)))
#define LAS __attribute__((address_space(3)))
typedef GAS unsigned long long gu64;

#define LD_AUX 2
#define Q_AUX LD_AUX
#define ST_AUX 2
#define NQ1 3
#define NQ2 3
#define DIAG 0

constexpr int NH = 32, NSEQ = 2048, DH = 192, ROWB = 384;
constexpr unsigned HEADB = NSEQ * ROWB;
constexpr unsigned TENB = NH * HEADB;
constexpr unsigned WS_EPOCH = 0;
constexpr unsigned WS_FLAG = 8192;
constexpr unsigned WS_PART = WS_FLAG + NH * 8 * 128;
constexpr unsigned WS_END = WS_PART + NH * 8 * 8 * 768;
constexpr unsigned WS_DIAG = 4u << 20;
constexpr int L_RED = 0;
constexpr int KSTR = 400;
constexpr int L_KP = 65536;
constexpr int L_VP = L_KP + 8 * KSTR;
constexpr int L_ZERO = L_VP + 2 * 4 * 24 * 16;
constexpr int L_END = L_ZERO + 16;
constexpr int LDS_BYTES = 84 * 1024;
static_assert(L_END <= LDS_BYTES, "lds");

__device__ __forceinline__ v4u bload(__amdgpu_buffer_rsrc_t r, unsigned voff, unsigned soff, const int aux_is_const_dummy = 0);
template <int AUX> __device__ __forceinline__ v4u ld16(__amdgpu_buffer_rsrc_t r, unsigned voff, unsigned soff) {
  return __builtin_amdgcn_raw_buffer_load_b128(r, voff, soff, AUX);
}
template <int AUX> __device__ __forceinline__ void st16(v4u x, __amdgpu_buffer_rsrc_t r, unsigned voff, unsigned soff) {
  __builtin_amdgcn_raw_buffer_store_b128(x, r, voff, soff, AUX);
}
__device__ __forceinline__ f32x4 mfma32(v4u a, v4u b, f32x4 c) {
  return __builtin_amdgcn_mfma_f32_16x16x32_bf16(__builtin_bit_cast(bf8, a), __builtin_bit_cast(bf8, b), c, 0, 0, 0);
}
__device__ __forceinline__ f32x4 mfma16(v2u a, v2u b, f32x4 c) {
  return __builtin_amdgcn_mfma_f32_16x16x16bf16_1k(__builtin_bit_cast(s4, a), __builtin_bit_cast(s4, b), c, 0, 0, 0);
}
__device__ __forceinline__ unsigned pk(float lo, float hi) {
  f32x2 v = {lo, hi};
  bf2 r = __builtin_convertvector(v, bf2);
  return __builtin_bit_cast(unsigned, r);
}
__device__ __forceinline__ float rbf(float x) {
  return __uint_as_float(pk(x, x) << 16);
}
#define SB() __builtin_amdgcn_sched_barrier(0)
#define STAMP(i)

__device__ __forceinline__ v4u colpair(const v4u (&x)[8], int jj) {
  const unsigned sel = (jj & 1) ? 0x07060302u : 0x05040100u;
  v4u o;
  o[0] = __builtin_amdgcn_perm(x[1][jj >> 1], x[0][jj >> 1], sel);
  o[1] = __builtin_amdgcn_perm(x[3][jj >> 1], x[2][jj >> 1], sel);
  o[2] = __builtin_amdgcn_perm(x[5][jj >> 1], x[4][jj >> 1], sel);
  o[3] = __builtin_amdgcn_perm(x[7][jj >> 1], x[6][jj >> 1], sel);
  return o;
}

__global__ void __launch_bounds__(512, 2)
linformer_fused(const char* __restrict__ q, const char* __restrict__ k, const char* __restrict__ v,
                const char* __restrict__ ep, const char* __restrict__ fp, char* __restrict__ out, char* ws) {
  extern __shared__ __attribute__((aligned(16))) char smem[];
  const int tid = threadIdx.x, l = tid & 63;
  const int w = __builtin_amdgcn_readfirstlane(tid >> 6);
  const int blk = blockIdx.x;
  const int h = (blk & 7) * 4 + ((blk >> 3) & 3), j = blk >> 5;
  const int kv = j >> 2, nq = j & 3;
  const int a = l >> 5, bb = (l >> 4) & 1, ee = (l >> 3) & 1, m = l & 7, kg = l >> 4, c = l & 15;
  STAMP(0);
  const __amdgpu_buffer_rsrc_t rKV = __builtin_amdgcn_make_buffer_rsrc((void*)((kv ? v : k) + (size_t)h * HEADB), 0, HEADB, 0x00020000);
  const __amdgpu_buffer_rsrc_t rE = __builtin_amdgcn_make_buffer_rsrc((void*)(kv ? fp : ep), 0, 8 * NSEQ * 2, 0x00020000);
  const __amdgpu_buffer_rsrc_t rQ = __builtin_amdgcn_make_buffer_rsrc((void*)(q + (size_t)h * HEADB), 0, HEADB, 0x00020000);
  const __amdgpu_buffer_rsrc_t rO = __builtin_amdgcn_make_buffer_rsrc((void*)(out + (size_t)h * HEADB), 0, HEADB, 0x00020000);
  const __amdgpu_buffer_rsrc_t rW = __builtin_amdgcn_make_buffer_rsrc((void*)ws, 0, WS_END, 0x00020000);

  const unsigned n0 = nq * 512 + w * 64;
  const unsigned offE = (m * NSEQ + 8 * kg) * 2;
  const v4u eA0 = ld16<0>(rE, offE, n0 * 2);
  const v4u eA1 = ld16<0>(rE, offE + 64, n0 * 2);
  const unsigned long long epoch = __hip_atomic_load((gu64*)(ws + WS_EPOCH + h * 256), __ATOMIC_RELAXED, __HIP_MEMORY_SCOPE_AGENT);
  SB();
  const unsigned offA = 8 * kg * ROWB + c * 16;
  const unsigned offB = (32 * ee + 8 * kg) * ROWB + 256 + m * 16;
  const unsigned sKV = n0 * ROWB;
  v4u xa[8], xb[8], xc[8];
#pragma unroll
  for (int i = 0; i < 8; ++i) xa[i] = ld16<LD_AUX>(rKV, offA + i * ROWB, sKV);
#pragma unroll
  for (int i = 0; i < 8; ++i) xb[i] = ld16<LD_AUX>(rKV, offA + i * ROWB, sKV + 32 * ROWB);
#pragma unroll
  for (int i = 0; i < 8; ++i) xc[i] = ld16<LD_AUX>(rKV, offB + i * ROWB, sKV);
  SB();
  const unsigned q0 = j * 256 + w * 32;
  const unsigned sQ = q0 * ROWB;
  const unsigned offQ0 = m * ROWB + (4 * ee + kg) * 16;
  const unsigned offQ1 = m * ROWB + (4 * (ee ^ 1) + kg) * 16;
  v4u xq[12];
#define QLOAD(n) xq[n] = ld16<Q_AUX>(rQ, (((n) / 3) & 1 ? offQ1 : offQ0) + ((n) % 3) * 128, sQ + ((n) / 3 == 0 ? 0 : (n) / 3 == 1 ? 8 : (n) / 3 == 2 ? 24 : 16) * ROWB)

  f32x4 accA[8], accB[8];
  const f32x4 z4 = {0.f, 0.f, 0.f, 0.f};
#pragma unroll
  for (int jj = 0; jj < 8; ++jj) accA[jj] = mfma32(eA0, colpair(xa, jj), z4);
  SB();
  STAMP(1);
#pragma unroll
  for (int n = 0; n < NQ1; ++n) QLOAD(n);
  SB();
#pragma unroll
  for (int jj = 0; jj < 8; ++jj) accA[jj] = mfma32(eA1, colpair(xb, jj), accA[jj]);
  v4u eB;
#pragma unroll
  for (int i = 0; i < 4; ++i) eB[i] = ee ? eA1[i] : eA0[i];
#pragma unroll
  for (int jj = 0; jj < 8; ++jj) accB[jj] = mfma32(eB, colpair(xc, jj), z4);
  {
    const bool isB = (a == ee);
    const int pos = isB ? (32 + 16 * a + 8 * bb + m) : (16 * bb + c);
    LAS f32x4* red = (LAS f32x4*)(smem + L_RED) + w * 512 + pos;
#pragma unroll
    for (int jj = 0; jj < 8; ++jj) {
      f32x4 x;
#pragma unroll
      for (int i = 0; i < 4; ++i) x[i] = isB ? accB[jj][i] : accA[jj][i];
      red[jj * 64] = x;
    }
    if (tid == 0) *(LAS v4u*)(smem + L_ZERO) = (v4u){0u, 0u, 0u, 0u};
  }
  const unsigned long long tag = epoch + 1ull;
  asm volatile("" :: "s"(__builtin_amdgcn_readfirstlane((unsigned)tag)) : "memory");
  STAMP(2);
  asm volatile("s_waitcnt lgkmcnt(0)" ::: "memory");
  __builtin_amdgcn_s_barrier();
  SB();
  f32x4 s;
  {
    const LAS f32x4* red = (const LAS f32x4*)(smem + L_RED) + w * 64 + l;
    f32x4 t[8];
#pragma unroll
    for (int i = 0; i < 8; ++i) t[i] = red[i * 512];
    s = ((t[0] + t[1]) + (t[2] + t[3])) + ((t[4] + t[5]) + (t[6] + t[7]));
#pragma unroll
    for (int i = 0; i < 4; ++i) {
      auto r = __builtin_amdgcn_permlane16_swap(__float_as_uint(s[i]), __float_as_uint(s[i]), false, false);
      const float f = __uint_as_float(r[0]) + __uint_as_float(r[1]);
      s[i] = l < 32 ? s[i] : f;
    }
  }
  const unsigned myPart = WS_PART + ((h * 8 + j) * 8 + w) * 768;
  {
    v4u u = {__float_as_uint(s[0]), __float_as_uint(s[1]), __float_as_uint(s[2]), __float_as_uint(s[3])};
    st16<16>(u, rW, l < 48 ? l * 16u : 0x80000000u, myPart);
  }
  SB();
#pragma unroll
  for (int n = NQ1; n < NQ1 + NQ2; ++n) QLOAD(n);
  SB();
  asm volatile("s_waitcnt vmcnt(%0)" :: "i"(NQ2) : "memory");
  STAMP(3);
  gu64* flags = (gu64*)(ws + WS_FLAG + (h * 8 + w) * 128);
  if (l == 0) __hip_atomic_store(flags + 2 * j, tag, __ATOMIC_RELAXED, __HIP_MEMORY_SCOPE_AGENT);
  for (unsigned spins = 0; spins < (1u << 16); ++spins) {
    const unsigned long long x = __hip_atomic_load(flags + 2 * (l & 7), __ATOMIC_RELAXED, __HIP_MEMORY_SCOPE_AGENT);
    if (__all(x == tag)) break;
    __builtin_amdgcn_s_sleep(1);
  }
  __builtin_amdgcn_fence(__ATOMIC_ACQUIRE, "wavefront");
  SB();
  STAMP(4);
  if (j == 0 && w == 0 && l == 0) __hip_atomic_store((gu64*)(ws + WS_EPOCH + h * 256), tag, __ATOMIC_RELAXED, __HIP_MEMORY_SCOPE_AGENT);
  v4u pp[8];
  {
    const unsigned vo = l < 48 ? l * 16u : 0x80000000u;
    const unsigned sb = WS_PART + (h * 64 + w) * 768;
#pragma unroll
    for (int i = 0; i < 8; ++i) pp[i] = ld16<16>(rW, vo, sb + i * 8 * 768);
  }
  SB();
#pragma unroll
  for (int n = NQ1 + NQ2; n < 12; ++n) QLOAD(n);
  SB();
  {
    f32x4 ks, vs;
#pragma unroll
    for (int i = 0; i < 4; ++i) {
      ks[i] = (__uint_as_float(pp[0][i]) + __uint_as_float(pp[1][i])) + (__uint_as_float(pp[2][i]) + __uint_as_float(pp[3][i]));
      vs[i] = (__uint_as_float(pp[4][i]) + __uint_as_float(pp[5][i])) + (__uint_as_float(pp[6][i]) + __uint_as_float(pp[7][i]));
    }
    const int pb = l < 32 ? (l >> 4) : ((l >> 3) & 1);
    const int cc = l < 32 ? (l & 15) : 16 + (l & 7);
    if (l < 48) {
      const unsigned k01 = pk(ks[0], ks[1]), k23 = pk(ks[2], ks[3]);
      LAS unsigned short* kp = (LAS unsigned short*)(smem + L_KP + (4 * pb) * KSTR + (8 * cc + w) * 2);
      kp[0] = (unsigned short)k01;
      kp[KSTR / 2] = (unsigned short)(k01 >> 16);
      kp[KSTR] = (unsigned short)k23;
      kp[3 * KSTR / 2] = (unsigned short)(k23 >> 16);
      *(LAS v2u*)(smem + L_VP + ((pb * 4 + (w >> 1)) * 24 + cc) * 16 + (w & 1) * 8) = (v2u){pk(vs[0], vs[1]), pk(vs[2], vs[3])};
    }
  }
  STAMP(5);
  asm volatile("s_waitcnt lgkmcnt(0)" ::: "memory");
  __builtin_amdgcn_s_barrier();
  SB();
  STAMP(6);
  f32x4 d0 = z4, d1 = z4, d2 = z4, d3 = z4;
#pragma unroll
  for (int t = 0; t < 3; ++t) {
    const v4u ka = *(const LAS v4u*)(smem + L_KP + m * KSTR + (64 * t + 32 * ee + 8 * kg) * 2);
    const v4u kb = *(const LAS v4u*)(smem + L_KP + m * KSTR + (64 * t + 32 * (ee ^ 1) + 8 * kg) * 2);
    d0 = mfma32(ka, xq[0 + t], d0);
    d1 = mfma32(ka, xq[3 + t], d1);
    d2 = mfma32(kb, xq[6 + t], d2);
    d3 = mfma32(kb, xq[9 + t], d3);
  }
  v4u vA[4], vB[4];
#pragma unroll
  for (int i = 0; i < 4; ++i) {
    vA[i] = *(const LAS v4u*)(smem + L_VP + ((bb * 4 + i) * 24 + c) * 16);
    vB[i] = *(const LAS v4u*)(smem + (a == ee ? L_VP + ((bb * 4 + i) * 24 + 16 + m) * 16 : L_ZERO));
  }
  float x[4];
#pragma unroll
  for (int i = 0; i < 4; ++i) {
    const float L = (a == ee) ? d0[i] : d1[i], Hh = (a == ee) ? d3[i] : d2[i];
    auto r = __builtin_amdgcn_permlane32_swap(__float_as_uint(L), __float_as_uint(Hh), false, false);
    const float o8 = __uint_as_float(__builtin_amdgcn_mov_dpp((int)r[1], 0x128, 0xf, 0xf, true));
    const float sc = __uint_as_float(r[0]) + o8;
    x[i] = rbf(rbf(sc) * 0.07216878364870322f);
  }
  float mx = fmaxf(fmaxf(x[0], x[1]), fmaxf(x[2], x[3]));
  {
    auto r = __builtin_amdgcn_permlane16_swap(__float_as_uint(mx), __float_as_uint(mx), false, false);
    mx = fmaxf(__uint_as_float(r[0]), __uint_as_float(r[1]));
  }
  float p[4];
#pragma unroll
  for (int i = 0; i < 4; ++i) p[i] = __builtin_amdgcn_exp2f((x[i] - mx) * 1.4426950408889634f);
  float sm = (p[0] + p[1]) + (p[2] + p[3]);
  {
    auto r = __builtin_amdgcn_permlane16_swap(__float_as_uint(sm), __float_as_uint(sm), false, false);
    sm = __uint_as_float(r[0]) + __uint_as_float(r[1]);
  }
  const float inv = __builtin_amdgcn_rcpf(sm);
  const v2u P = {pk(p[0] * inv, p[1] * inv), pk(p[2] * inv, p[3] * inv)};
  const v2u Plo = {a ? 0u : P[0], a ? 0u : P[1]}, Phi = {a ? P[0] : 0u, a ? P[1] : 0u};
  const unsigned offOA = 4 * kg * ROWB + c * 16;
  const unsigned offOB = (16 * ee + 4 * kg) * ROWB + 256 + m * 16;
#define PVTILE(PA, VB, VOFF, SOFF) do { f32x4 o_[8]; \
    _Pragma("unroll") for (int jj = 0; jj < 8; ++jj) { const v2u bv = {VB[jj >> 1][(jj & 1) * 2], VB[jj >> 1][(jj & 1) * 2 + 1]}; o_[jj] = mfma16(PA, bv, z4); } \
    _Pragma("unroll") for (int i = 0; i < 4; ++i) { \
      const v4u u = {pk(o_[0][i], o_[1][i]), pk(o_[2][i], o_[3][i]), pk(o_[4][i], o_[5][i]), pk(o_[6][i], o_[7][i])}; \
      st16<ST_AUX>(u, rO, (VOFF) + i * ROWB, (SOFF)); } } while (0)
  PVTILE(Plo, vA, offOA, sQ);
  PVTILE(P, vB, offOB, sQ);
  PVTILE(Phi, vA, offOA, sQ + 16 * ROWB);
}

extern "C" void kernel_launch(void* const* d_in, const int* in_sizes, int n_in, void* d_out, int out_size,
                              void* d_ws, size_t ws_size, hipStream_t stream) {
  (void)in_sizes; (void)n_in; (void)out_size; (void)ws_size;
  hipFuncSetAttribute((const void*)linformer_fused, hipFuncAttributeMaxDynamicSharedMemorySize, LDS_BYTES);
  linformer_fused<<<256, 512, LDS_BYTES, stream>>>((const char*)d_in[0], (const char*)d_in[1], (const char*)d_in[2],
                                                   (const char*)d_in[3], (const char*)d_in[4], (char*)d_out, (char*)d_ws);
}
```

```cpp
#include <hip/hip_runtime.h>
#include <hip/hip_bf16.h>
#include <cstdio>

typedef unsigned v4u __attribute__((ext_vector_type(4)));
typedef unsigned v2u __attribute__((ext_vector_type(2)));
typedef float f32x4 __attribute__((ext_vector_type(4)));
typedef float f32x2 __attribute__((ext_vector_type(2)));
typedef __bf16 bf8 __attribute__((ext_vector_type(8)));
typedef __bf16 bf2 __attribute__((ext_vector_type(2)));
typedef short s4 __attribute__((ext_vector_type(4)));
#define GAS __attribute__((address_space(1)))
#define LAS __attribute__((address_space(3)))
typedef GAS unsigned long long gu64;

#define LD_AUX 2
#define Q_AUX LD_AUX
#define ST_AUX 2
#define NQ1 3
#define NQ2 3
#define DIAG 1

constexpr int NH = 32, NSEQ = 2048, DH = 192, ROWB = 384;
constexpr unsigned HEADB = NSEQ * ROWB;
constexpr unsigned TENB = NH * HEADB;
constexpr unsigned WS_EPOCH = 0;
constexpr unsigned WS_FLAG = 8192;
constexpr unsigned WS_PART = WS_FLAG + NH * 8 * 128;
constexpr unsigned WS_END = WS_PART + NH * 8 * 8 * 768;
constexpr unsigned WS_DIAG = 4u << 20;
constexpr int L_RED = 0;
constexpr int KSTR = 400;
constexpr int L_KP = 65536;
constexpr int L_VP = L_KP + 8 * KSTR;
constexpr int L_ZERO = L_VP + 2 * 4 * 24 * 16;
constexpr int L_END = L_ZERO + 16;
constexpr int LDS_BYTES = 84 * 1024;
static_assert(L_END <= LDS_BYTES, "lds");

__device__ __forceinline__ v4u bload(__amdgpu_buffer_rsrc_t r, unsigned voff, unsigned soff, const int aux_is_const_dummy = 0);
template <int AUX> __device__ __forceinline__ v4u ld16(__amdgpu_buffer_rsrc_t r, unsigned voff, unsigned soff) {
  return __builtin_amdgcn_raw_buffer_load_b128(r, voff, soff, AUX);
}
template <int AUX> __device__ __forceinline__ void st16(v4u x, __amdgpu_buffer_rsrc_t r, unsigned voff, unsigned soff) {
  __builtin_amdgcn_raw_buffer_store_b128(x, r, voff, soff, AUX);
}
__device__ __forceinline__ f32x4 mfma32(v4u a, v4u b, f32x4 c) {
  return __builtin_amdgcn_mfma_f32_16x16x32_bf16(__builtin_bit_cast(bf8, a), __builtin_bit_cast(bf8, b), c, 0, 0, 0);
}
__device__ __forceinline__ f32x4 mfma16(v2u a, v2u b, f32x4 c) {
  return __builtin_amdgcn_mfma_f32_16x16x16bf16_1k(__builtin_bit_cast(s4, a), __builtin_bit_cast(s4, b), c, 0, 0, 0);
}
__device__ __forceinline__ unsigned pk(float lo, float hi) {
  f32x2 v = {lo, hi};
  bf2 r = __builtin_convertvector(v, bf2);
  return __builtin_bit_cast(unsigned, r);
}
__device__ __forceinline__ float rbf(float x) {
  return __uint_as_float(pk(x, x) << 16);
}
#define SB() __builtin_amdgcn_sched_barrier(0)
#define STAMP(i) do { SB(); unsigned long long t_; asm volatile("s_memrealtime %0\n\ts_waitcnt lgkmcnt(0)" : "=s"(t_) :: "memory"); st_[i] = t_; SB(); } while (0)

__device__ __forceinline__ v4u colpair(const v4u (&x)[8], int jj) {
  const unsigned sel = (jj & 1) ? 0x07060302u : 0x05040100u;
  v4u o;
  o[0] = __builtin_amdgcn_perm(x[1][jj >> 1], x[0][jj >> 1], sel);
  o[1] = __builtin_amdgcn_perm(x[3][jj >> 1], x[2][jj >> 1], sel);
  o[2] = __builtin_amdgcn_perm(x[5][jj >> 1], x[4][jj >> 1], sel);
  o[3] = __builtin_amdgcn_perm(x[7][jj >> 1], x[6][jj >> 1], sel);
  return o;
}

__global__ void __launch_bounds__(512, 2)
linformer_fused(const char* __restrict__ q, const char* __restrict__ k, const char* __restrict__ v,
                const char* __restrict__ ep, const char* __restrict__ fp, char* __restrict__ out, char* ws) {
  extern __shared__ __attribute__((aligned(16))) char smem[];
  const int tid = threadIdx.x, l = tid & 63;
  const int w = __builtin_amdgcn_readfirstlane(tid >> 6);
  const int blk = blockIdx.x;
  const int h = (blk & 7) * 4 + ((blk >> 3) & 3), j = blk >> 5;
  const int kv = j >> 2, nq = j & 3;
  const int a = l >> 5, bb = (l >> 4) & 1, ee = (l >> 3) & 1, m = l & 7, kg = l >> 4, c = l & 15;
  unsigned long long st_[10];
  STAMP(0);
  const __amdgpu_buffer_rsrc_t rKV = __builtin_amdgcn_make_buffer_rsrc((void*)((kv ? v : k) + (size_t)h * HEADB), 0, HEADB, 0x00020000);
  const __amdgpu_buffer_rsrc_t rE = __builtin_amdgcn_make_buffer_rsrc((void*)(kv ? fp : ep), 0, 8 * NSEQ * 2, 0x00020000);
  const __amdgpu_buffer_rsrc_t rQ = __builtin_amdgcn_make_buffer_rsrc((void*)(q + (size_t)h * HEADB), 0, HEADB, 0x00020000);
  const __amdgpu_buffer_rsrc_t rO = __builtin_amdgcn_make_buffer_rsrc((void*)(out + (size_t)h * HEADB), 0, HEADB, 0x00020000);
  const __amdgpu_buffer_rsrc_t rW = __builtin_amdgcn_make_buffer_rsrc((void*)ws, 0, WS_END, 0x00020000);

  const unsigned n0 = nq * 512 + w * 64;
  const unsigned offE = (m * NSEQ + 8 * kg) * 2;
  const v4u eA0 = ld16<0>(rE, offE, n0 * 2);
  const v4u eA1 = ld16<0>(rE, offE + 64, n0 * 2);
  const unsigned long long epoch = __hip_atomic_load((gu64*)(ws + WS_EPOCH + h * 256), __ATOMIC_RELAXED, __HIP_MEMORY_SCOPE_AGENT);
  SB();
  const unsigned offA = 8 * kg * ROWB + c * 16;
  const unsigned offB = (32 * ee + 8 * kg) * ROWB + 256 + m * 16;
  const unsigned sKV = n0 * ROWB;
  v4u xa[8], xb[8], xc[8];
#pragma unroll
  for (int i = 0; i < 8; ++i) xa[i] = ld16<LD_AUX>(rKV, offA + i * ROWB, sKV);
#pragma unroll
  for (int i = 0; i < 8; ++i) xb[i] = ld16<LD_AUX>(rKV, offA + i * ROWB, sKV + 32 * ROWB);
#pragma unroll
  for (int i = 0; i < 8; ++i) xc[i] = ld16<LD_AUX>(rKV, offB + i * ROWB, sKV);
  SB();
  const unsigned q0 = j * 256 + w * 32;
  const unsigned sQ = q0 * ROWB;
  const unsigned offQ0 = m * ROWB + (4 * ee + kg) * 16;
  const unsigned offQ1 = m * ROWB + (4 * (ee ^ 1) + kg) * 16;
  v4u xq[12];
#define QLOAD(n) xq[n] = ld16<Q_AUX>(rQ, (((n) / 3) & 1 ? offQ1 : offQ0) + ((n) % 3) * 128, sQ + ((n) / 3 == 0 ? 0 : (n) / 3 == 1 ? 8 : (n) / 3 == 2 ? 24 : 16) * ROWB)

  f32x4 accA[8], accB[8];
  const f32x4 z4 = {0.f, 0.f, 0.f, 0.f};
#pragma unroll
  for (int jj = 0; jj < 8; ++jj) accA[jj] = mfma32(eA0, colpair(xa, jj), z4);
  SB();
  STAMP(1);
#pragma unroll
  for (int n = 0; n < NQ1; ++n) QLOAD(n);
  SB();
#pragma unroll
  for (int jj = 0; jj < 8; ++jj) accA[jj] = mfma32(eA1, colpair(xb, jj), accA[jj]);
  v4u eB;
#pragma unroll
  for (int i = 0; i < 4; ++i) eB[i] = ee ? eA1[i] : eA0[i];
#pragma unroll
  for (int jj = 0; jj < 8; ++jj) accB[jj] = mfma32(eB, colpair(xc, jj), z4);
  {
    const bool isB = (a == ee);
    const int pos = isB ? (32 + 16 * a + 8 * bb + m) : (16 * bb + c);
    LAS f32x4* red = (LAS f32x4*)(smem + L_RED) + w * 512 + pos;
#pragma unroll
    for (int jj = 0; jj < 8; ++jj) {
      f32x4 x;
#pragma unroll
      for (int i = 0; i < 4; ++i) x[i] = isB ? accB[jj][i] : accA[jj][i];
      red[jj * 64] = x;
    }
    if (tid == 0) *(LAS v4u*)(smem + L_ZERO) = (v4u){0u, 0u, 0u, 0u};
  }
  const unsigned long long tag = epoch + 1ull;
  asm volatile("" :: "s"(__builtin_amdgcn_readfirstlane((unsigned)tag)) : "memory");
  STAMP(2);
  asm volatile("s_waitcnt lgkmcnt(0)" ::: "memory");
  __builtin_amdgcn_s_barrier();
  SB();
  f32x4 s;
  {
    const LAS f32x4* red = (const LAS f32x4*)(smem + L_RED) + w * 64 + l;
    f32x4 t[8];
#pragma unroll
    for (int i = 0; i < 8; ++i) t[i] = red[i * 512];
    s = ((t[0] + t[1]) + (t[2] + t[3])) + ((t[4] + t[5]) + (t[6] + t[7]));
#pragma unroll
    for (int i = 0; i < 4; ++i) {
      auto r = __builtin_amdgcn_permlane16_swap(__float_as_uint(s[i]), __float_as_uint(s[i]), false, false);
      const float f = __uint_as_float(r[0]) + __uint_as_float(r[1]);
      s[i] = l < 32 ? s[i] : f;
    }
  }
  const unsigned myPart = WS_PART + ((h * 8 + j) * 8 + w) * 768;
  {
    v4u u = {__float_as_uint(s[0]), __float_as_uint(s[1]), __float_as_uint(s[2]), __float_as_uint(s[3])};
    st16<16>(u, rW, l < 48 ? l * 16u : 0x80000000u, myPart);
  }
  SB();
#pragma unroll
  for (int n = NQ1; n < NQ1 + NQ2; ++n) QLOAD(n);
  SB();
  asm volatile("s_waitcnt vmcnt(%0)" :: "i"(NQ2) : "memory");
  STAMP(3);
  gu64* flags = (gu64*)(ws + WS_FLAG + (h * 8 + w) * 128);
  if (l == 0) __hip_atomic_store(flags + 2 * j, tag, __ATOMIC_RELAXED, __HIP_MEMORY_SCOPE_AGENT);
  for (unsigned spins = 0; spins < (1u << 16); ++spins) {
    const unsigned long long x = __hip_atomic_load(flags + 2 * (l & 7), __ATOMIC_RELAXED, __HIP_MEMORY_SCOPE_AGENT);
    if (__all(x == tag)) break;
    __builtin_amdgcn_s_sleep(1);
  }
  __builtin_amdgcn_fence(__ATOMIC_ACQUIRE, "wavefront");
  SB();
  STAMP(4);
  if (j == 0 && w == 0 && l == 0) __hip_atomic_store((gu64*)(ws + WS_EPOCH + h * 256), tag, __ATOMIC_RELAXED, __HIP_MEMORY_SCOPE_AGENT);
  v4u pp[8];
  {
    const unsigned vo = l < 48 ? l * 16u : 0x80000000u;
    const unsigned sb = WS_PART + (h * 64 + w) * 768;
#pragma unroll
    for (int i = 0; i < 8; ++i) pp[i] = ld16<16>(rW, vo, sb + i * 8 * 768);
  }
  SB();
#pragma unroll
  for (int n = NQ1 + NQ2; n < 12; ++n) QLOAD(n);
  SB();
  {
    f32x4 ks, vs;
#pragma unroll
    for (int i = 0; i < 4; ++i) {
      ks[i] = (__uint_as_float(pp[0][i]) + __uint_as_float(pp[1][i])) + (__uint_as_float(pp[2][i]) + __uint_as_float(pp[3][i]));
      vs[i] = (__uint_as_float(pp[4][i]) + __uint_as_float(pp[5][i])) + (__uint_as_float(pp[6][i]) + __uint_as_float(pp[7][i]));
    }
    const int pb = l < 32 ? (l >> 4) : ((l >> 3) & 1);
    const int cc = l < 32 ? (l & 15) : 16 + (l & 7);
    if (l < 48) {
      const unsigned k01 = pk(ks[0], ks[1]), k23 = pk(ks[2], ks[3]);
      LAS unsigned short* kp = (LAS unsigned short*)(smem + L_KP + (4 * pb) * KSTR + (8 * cc + w) * 2);
      kp[0] = (unsigned short)k01;
      kp[KSTR / 2] = (unsigned short)(k01 >> 16);
      kp[KSTR] = (unsigned short)k23;
      kp[3 * KSTR / 2] = (unsigned short)(k23 >> 16);
      *(LAS v2u*)(smem + L_VP + ((pb * 4 + (w >> 1)) * 24 + cc) * 16 + (w & 1) * 8) = (v2u){pk(vs[0], vs[1]), pk(vs[2], vs[3])};
    }
  }
  STAMP(5);
  asm volatile("s_waitcnt lgkmcnt(0)" ::: "memory");
  __builtin_amdgcn_s_barrier();
  SB();
  STAMP(6);
  f32x4 d0 = z4, d1 = z4, d2 = z4, d3 = z4;
#pragma unroll
  for (int t = 0; t < 3; ++t) {
    const v4u ka = *(const LAS v4u*)(smem + L_KP + m * KSTR + (64 * t + 32 * ee + 8 * kg) * 2);
    const v4u kb = *(const LAS v4u*)(smem + L_KP + m * KSTR + (64 * t + 32 * (ee ^ 1) + 8 * kg) * 2);
    d0 = mfma32(ka, xq[0 + t], d0);
    d1 = mfma32(ka, xq[3 + t], d1);
    d2 = mfma32(kb, xq[6 + t], d2);
    d3 = mfma32(kb, xq[9 + t], d3);
  }
  v4u vA[4], vB[4];
#pragma unroll
  for (int i = 0; i < 4; ++i) {
    vA[i] = *(const LAS v4u*)(smem + L_VP + ((bb * 4 + i) * 24 + c) * 16);
    vB[i] = *(const LAS v4u*)(smem + (a == ee ? L_VP + ((bb * 4 + i) * 24 + 16 + m) * 16 : L_ZERO));
  }
  float x[4];
#pragma unroll
  for (int i = 0; i < 4; ++i) {
    const float L = (a == ee) ? d0[i] : d1[i], Hh = (a == ee) ? d3[i] : d2[i];
    auto r = __builtin_amdgcn_permlane32_swap(__float_as_uint(L), __float_as_uint(Hh), false, false);
    const float o8 = __uint_as_float(__builtin_amdgcn_mov_dpp((int)r[1], 0x128, 0xf, 0xf, true));
    const float sc = __uint_as_float(r[0]) + o8;
    x[i] = rbf(rbf(sc) * 0.07216878364870322f);
  }
  float mx = fmaxf(fmaxf(x[0], x[1]), fmaxf(x[2], x[3]));
  {
    auto r = __builtin_amdgcn_permlane16_swap(__float_as_uint(mx), __float_as_uint(mx), false, false);
    mx = fmaxf(__uint_as_float(r[0]), __uint_as_float(r[1]));
  }
  float p[4];
#pragma unroll
  for (int i = 0; i < 4; ++i) p[i] = __builtin_amdgcn_exp2f((x[i] - mx) * 1.4426950408889634f);
  float sm = (p[0] + p[1]) + (p[2] + p[3]);
  {
    auto r = __builtin_amdgcn_permlane16_swap(__float_as_uint(sm), __float_as_uint(sm), false, false);
    sm = __uint_as_float(r[0]) + __uint_as_float(r[1]);
  }
  const float inv = __builtin_amdgcn_rcpf(sm);
  const v2u P = {pk(p[0] * inv, p[1] * inv), pk(p[2] * inv, p[3] * inv)};
  const v2u Plo = {a ? 0u : P[0], a ? 0u : P[1]}, Phi = {a ? P[0] : 0u, a ? P[1] : 0u};
  const unsigned offOA = 4 * kg * ROWB + c * 16;
  const unsigned offOB = (16 * ee + 4 * kg) * ROWB + 256 + m * 16;
#define PVTILE(PA, VB, VOFF, SOFF) do { f32x4 o_[8]; \
    _Pragma("unroll") for (int jj = 0; jj < 8; ++jj) { const v2u bv = {VB[jj >> 1][(jj & 1) * 2], VB[jj >> 1][(jj & 1) * 2 + 1]}; o_[jj] = mfma16(PA, bv, z4); } \
    _Pragma("unroll") for (int i = 0; i < 4; ++i) { \
      const v4u u = {pk(o_[0][i], o_[1][i]), pk(o_[2][i], o_[3][i]), pk(o_[4][i], o_[5][i]), pk(o_[6][i], o_[7][i])}; \
      st16<ST_AUX>(u, rO, (VOFF) + i * ROWB, (SOFF)); } } while (0)
  PVTILE(Plo, vA, offOA, sQ);
  PVTILE(P, vB, offOB, sQ);
  PVTILE(Phi, vA, offOA, sQ + 16 * ROWB);
  STAMP(7);
  asm volatile("s_waitcnt vmcnt(0)" ::: "memory");
  STAMP(8);
  if (l == 0 && w == 0 && (blk % 37) == 0) printf("blk %d: t0 %llu d1 %d d2 %d d3 %d d4 %d d5 %d d6 %d d7 %d d8 %d\n", blk, st_[0] % 100000ull, (int)(st_[1]-st_[0]), (int)(st_[2]-st_[0]), (int)(st_[3]-st_[0]), (int)(st_[4]-st_[0]), (int)(st_[5]-st_[0]), (int)(st_[6]-st_[0]), (int)(st_[7]-st_[0]), (int)(st_[8]-st_[0]));
  if (l == 0) {
    unsigned long long* dd = (unsigned long long*)(ws + WS_DIAG) + (size_t)(blk * 8 + w) * 16;
#pragma unroll
    for (int i = 0; i < 9; ++i) dd[i] = st_[i];
  }
}

extern "C" void kernel_launch(void* const* d_in, const int* in_sizes, int n_in, void* d_out, int out_size,
                              void* d_ws, size_t ws_size, hipStream_t stream) {
  (void)in_sizes; (void)n_in; (void)out_size; (void)ws_size;
  hipFuncSetAttribute((const void*)linformer_fused, hipFuncAttributeMaxDynamicSharedMemorySize, LDS_BYTES);
  linformer_fused<<<256, 512, LDS_BYTES, stream>>>((const char*)d_in[0], (const char*)d_in[1], (const char*)d_in[2],
                                                   (const char*)d_in[3], (const char*)d_in[4], (char*)d_out, (char*)d_ws);
}
```

```cpp
#include <hip/hip_runtime.h>
#include <hip/hip_bf16.h>

typedef unsigned v4u __attribute__((ext_vector_type(4)));
typedef unsigned v2u __attribute__((ext_vector_type(2)));
typedef float f32x4 __attribute__((ext_vector_type(4)));
typedef float f32x2 __attribute__((ext_vector_type(2)));
typedef __bf16 bf8 __attribute__((ext_vector_type(8)));
typedef __bf16 bf2 __attribute__((ext_vector_type(2)));
typedef short s4 __attribute__((ext_vector_type(4)));
#define GAS __attribute__((address_space(1)))
#define LAS __attribute__((address_space(3)))
typedef GAS unsigned long long gu64;

#define LD_AUX 2
#define Q_AUX LD_AUX
#define ST_AUX 2
#define NQ1 3
#define NQ2 3
#define DIAG 0

constexpr int NH = 32, NSEQ = 2048, DH = 192, ROWB = 384;
constexpr unsigned HEADB = NSEQ * ROWB;
constexpr unsigned TENB = NH * HEADB;
constexpr unsigned WS_EPOCH = 0;
constexpr unsigned WS_FLAG = 8192;
constexpr unsigned WS_PART = WS_FLAG + NH * 8 * 128;
constexpr unsigned WS_END = WS_PART + NH * 8 * 8 * 768;
constexpr unsigned WS_DIAG = 4u << 20;
constexpr int L_RED = 0;
constexpr int KSTR = 400;
constexpr int L_KP = 65536;
constexpr int L_VP = L_KP + 8 * KSTR;
constexpr int L_ZERO = L_VP + 2 * 4 * 24 * 16;
constexpr int L_END = L_ZERO + 16;
constexpr int LDS_BYTES = 160 * 1024;
static_assert(L_END <= LDS_BYTES, "lds");

__device__ __forceinline__ v4u bload(__amdgpu_buffer_rsrc_t r, unsigned voff, unsigned soff, const int aux_is_const_dummy = 0);
template <int AUX> __device__ __forceinline__ v4u ld16(__amdgpu_buffer_rsrc_t r, unsigned voff, unsigned soff) {
  return __builtin_amdgcn_raw_buffer_load_b128(r, voff, soff, AUX);
}
template <int AUX> __device__ __forceinline__ void st16(v4u x, __amdgpu_buffer_rsrc_t r, unsigned voff, unsigned soff) {
  __builtin_amdgcn_raw_buffer_store_b128(x, r, voff, soff, AUX);
}
__device__ __forceinline__ f32x4 mfma32(v4u a, v4u b, f32x4 c) {
  return __builtin_amdgcn_mfma_f32_16x16x32_bf16(__builtin_bit_cast(bf8, a), __builtin_bit_cast(bf8, b), c, 0, 0, 0);
}
__device__ __forceinline__ f32x4 mfma16(v2u a, v2u b, f32x4 c) {
  return __builtin_amdgcn_mfma_f32_16x16x16bf16_1k(__builtin_bit_cast(s4, a), __builtin_bit_cast(s4, b), c, 0, 0, 0);
}
__device__ __forceinline__ unsigned pk(float lo, float hi) {
  f32x2 v = {lo, hi};
  bf2 r = __builtin_convertvector(v, bf2);
  return __builtin_bit_cast(unsigned, r);
}
__device__ __forceinline__ float rbf(float x) {
  return __uint_as_float(pk(x, x) << 16);
}
#define SB() __builtin_amdgcn_sched_barrier(0)
#define STAMP(i)

__device__ __forceinline__ v4u colpair(const v4u (&x)[8], int jj) {
  const unsigned sel = (jj & 1) ? 0x07060302u : 0x05040100u;
  v4u o;
  o[0] = __builtin_amdgcn_perm(x[1][jj >> 1], x[0][jj >> 1], sel);
  o[1] = __builtin_amdgcn_perm(x[3][jj >> 1], x[2][jj >> 1], sel);
  o[2] = __builtin_amdgcn_perm(x[5][jj >> 1], x[4][jj >> 1], sel);
  o[3] = __builtin_amdgcn_perm(x[7][jj >> 1], x[6][jj >> 1], sel);
  return o;
}

__global__ void __launch_bounds__(512, 2)
linformer_fused(const char* __restrict__ q, const char* __restrict__ k, const char* __restrict__ v,
                const char* __restrict__ ep, const char* __restrict__ fp, char* __restrict__ out, char* ws) {
  extern __shared__ __attribute__((aligned(16))) char smem[];
  const int tid = threadIdx.x, l = tid & 63;
  const int w = __builtin_amdgcn_readfirstlane(tid >> 6);
  const int blk = blockIdx.x;
  const int h = (blk & 7) * 4 + ((blk >> 3) & 3), j = blk >> 5;
  const int kv = j >> 2, nq = j & 3;
  const int a = l >> 5, bb = (l >> 4) & 1, ee = (l >> 3) & 1, m = l & 7, kg = l >> 4, c = l & 15;
  STAMP(0);
  const __amdgpu_buffer_rsrc_t rKV = __builtin_amdgcn_make_buffer_rsrc((void*)((kv ? v : k) + (size_t)h * HEADB), 0, HEADB, 0x00020000);
  const __amdgpu_buffer_rsrc_t rE = __builtin_amdgcn_make_buffer_rsrc((void*)(kv ? fp : ep), 0, 8 * NSEQ * 2, 0x00020000);
  const __amdgpu_buffer_rsrc_t rQ = __builtin_amdgcn_make_buffer_rsrc((void*)(q + (size_t)h * HEADB), 0, HEADB, 0x00020000);
  const __amdgpu_buffer_rsrc_t rO = __builtin_amdgcn_make_buffer_rsrc((void*)(out + (size_t)h * HEADB), 0, HEADB, 0x00020000);
  const __amdgpu_buffer_rsrc_t rW = __builtin_amdgcn_make_buffer_rsrc((void*)ws, 0, WS_END, 0x00020000);

  const unsigned n0 = nq * 512 + w * 64;
  const unsigned offE = (m * NSEQ + 8 * kg) * 2;
  const v4u eA0 = ld16<0>(rE, offE, n0 * 2);
  const v4u eA1 = ld16<0>(rE, offE + 64, n0 * 2);
  const unsigned long long epoch = __hip_atomic_load((gu64*)(ws + WS_EPOCH + h * 256), __ATOMIC_RELAXED, __HIP_MEMORY_SCOPE_AGENT);
  SB();
  const unsigned offA = 8 * kg * ROWB + c * 16;
  const unsigned offB = (32 * ee + 8 * kg) * ROWB + 256 + m * 16;
  const unsigned sKV = n0 * ROWB;
  v4u xa[8], xb[8], xc[8];
#pragma unroll
  for (int i = 0; i < 8; ++i) xa[i] = ld16<LD_AUX>(rKV, offA + i * ROWB, sKV);
#pragma unroll
  for (int i = 0; i < 8; ++i) xb[i] = ld16<LD_AUX>(rKV, offA + i * ROWB, sKV + 32 * ROWB);
#pragma unroll
  for (int i = 0; i < 8; ++i) xc[i] = ld16<LD_AUX>(rKV, offB + i * ROWB, sKV);
  SB();
  const unsigned q0 = j * 256 + w * 32;
  const unsigned sQ = q0 * ROWB;
  const unsigned offQ0 = m * ROWB + (4 * ee + kg) * 16;
  const unsigned offQ1 = m * ROWB + (4 * (ee ^ 1) + kg) * 16;
  v4u xq[12];
#define QLOAD(n) xq[n] = ld16<Q_AUX>(rQ, (((n) / 3) & 1 ? offQ1 : offQ0) + ((n) % 3) * 128, sQ + ((n) / 3 == 0 ? 0 : (n) / 3 == 1 ? 8 : (n) / 3 == 2 ? 24 : 16) * ROWB)

  f32x4 accA[8], accB[8];
  const f32x4 z4 = {0.f, 0.f, 0.f, 0.f};
#pragma unroll
  for (int jj = 0; jj < 8; ++jj) accA[jj] = mfma32(eA0, colpair(xa, jj), z4);
  SB();
  STAMP(1);
#pragma unroll
  for (int n = 0; n < NQ1; ++n) QLOAD(n);
  SB();
#pragma unroll
  for (int jj = 0; jj < 8; ++jj) accA[jj] = mfma32(eA1, colpair(xb, jj), accA[jj]);
  v4u eB;
#pragma unroll
  for (int i = 0; i < 4; ++i) eB[i] = ee ? eA1[i] : eA0[i];
#pragma unroll
  for (int jj = 0; jj < 8; ++jj) accB[jj] = mfma32(eB, colpair(xc, jj), z4);
  {
    const bool isB = (a == ee);
    const int pos = isB ? (32 + 16 * a + 8 * bb + m) : (16 * bb + c);
    LAS f32x4* red = (LAS f32x4*)(smem + L_RED) + w * 512 + pos;
#pragma unroll
    for (int jj = 0; jj < 8; ++jj) {
      f32x4 x;
#pragma unroll
      for (int i = 0; i < 4; ++i) x[i] = isB ? accB[jj][i] : accA[jj][i];
      red[jj * 64] = x;
    }
    if (tid == 0) *(LAS v4u*)(smem + L_ZERO) = (v4u){0u, 0u, 0u, 0u};
  }
  const unsigned long long tag = epoch + 1ull;
  asm volatile("" :: "s"(__builtin_amdgcn_readfirstlane((unsigned)tag)) : "memory");
  STAMP(2);
  asm volatile("s_waitcnt lgkmcnt(0)" ::: "memory");
  __builtin_amdgcn_s_barrier();
  SB();
  f32x4 s;
  {
    const LAS f32x4* red = (const LAS f32x4*)(smem + L_RED) + w * 64 + l;
    f32x4 t[8];
#pragma unroll
    for (int i = 0; i < 8; ++i) t[i] = red[i * 512];
    s = ((t[0] + t[1]) + (t[2] + t[3])) + ((t[4] + t[5]) + (t[6] + t[7]));
#pragma unroll
    for (int i = 0; i < 4; ++i) {
      auto r = __builtin_amdgcn_permlane16_swap(__float_as_uint(s[i]), __float_as_uint(s[i]), false, false);
      const float f = __uint_as_float(r[0]) + __uint_as_float(r[1]);
      s[i] = l < 32 ? s[i] : f;
    }
  }
  const unsigned myPart = WS_PART + ((h * 8 + j) * 8 + w) * 768;
  {
    v4u u = {__float_as_uint(s[0]), __float_as_uint(s[1]), __float_as_uint(s[2]), __float_as_uint(s[3])};
    st16<16>(u, rW, l < 48 ? l * 16u : 0x80000000u, myPart);
  }
  SB();
#pragma unroll
  for (int n = NQ1; n < NQ1 + NQ2; ++n) QLOAD(n);
  SB();
  asm volatile("s_waitcnt vmcnt(%0)" :: "i"(NQ2) : "memory");
  STAMP(3);
  gu64* flags = (gu64*)(ws + WS_FLAG + (h * 8 + w) * 128);
  if (l == 0) __hip_atomic_store(flags + 2 * j, tag, __ATOMIC_RELAXED, __HIP_MEMORY_SCOPE_AGENT);
  for (unsigned spins = 0; spins < (1u << 16); ++spins) {
    const unsigned long long x = __hip_atomic_load(flags + 2 * (l & 7), __ATOMIC_RELAXED, __HIP_MEMORY_SCOPE_AGENT);
    if (__all(x == tag)) break;
    __builtin_amdgcn_s_sleep(1);
  }
  __builtin_amdgcn_fence(__ATOMIC_ACQUIRE, "wavefront");
  SB();
  STAMP(4);
  if (j == 0 && w == 0 && l == 0) __hip_atomic_store((gu64*)(ws + WS_EPOCH + h * 256), tag, __ATOMIC_RELAXED, __HIP_MEMORY_SCOPE_AGENT);
  v4u pp[8];
  {
    const unsigned vo = l < 48 ? l * 16u : 0x80000000u;
    const unsigned sb = WS_PART + (h * 64 + w) * 768;
#pragma unroll
    for (int i = 0; i < 8; ++i) pp[i] = ld16<16>(rW, vo, sb + i * 8 * 768);
  }
  SB();
#pragma unroll
  for (int n = NQ1 + NQ2; n < 12; ++n) QLOAD(n);
  SB();
  {
    f32x4 ks, vs;
#pragma unroll
    for (int i = 0; i < 4; ++i) {
      ks[i] = (__uint_as_float(pp[0][i]) + __uint_as_float(pp[1][i])) + (__uint_as_float(pp[2][i]) + __uint_as_float(pp[3][i]));
      vs[i] = (__uint_as_float(pp[4][i]) + __uint_as_float(pp[5][i])) + (__uint_as_float(pp[6][i]) + __uint_as_float(pp[7][i]));
    }
    const int pb = l < 32 ? (l >> 4) : ((l >> 3) & 1);
    const int cc = l < 32 ? (l & 15) : 16 + (l & 7);
    if (l < 48) {
      const unsigned k01 = pk(ks[0], ks[1]), k23 = pk(ks[2], ks[3]);
      LAS unsigned short* kp = (LAS unsigned short*)(smem + L_KP + (4 * pb) * KSTR + (8 * cc + w) * 2);
      kp[0] = (unsigned short)k01;
      kp[KSTR / 2] = (unsigned short)(k01 >> 16);
      kp[KSTR] = (unsigned short)k23;
      kp[3 * KSTR / 2] = (unsigned short)(k23 >> 16);
      *(LAS v2u*)(smem + L_VP + ((pb * 4 + (w >> 1)) * 24 + cc) * 16 + (w & 1) * 8) = (v2u){pk(vs[0], vs[1]), pk(vs[2], vs[3])};
    }
  }
  STAMP(5);
  asm volatile("s_waitcnt lgkmcnt(0)" ::: "memory");
  __builtin_amdgcn_s_barrier();
  SB();
  STAMP(6);
  f32x4 d0 = z4, d1 = z4, d2 = z4, d3 = z4;
#pragma unroll
  for (int t = 0; t < 3; ++t) {
    const v4u ka = *(const LAS v4u*)(smem + L_KP + m * KSTR + (64 * t + 32 * ee + 8 * kg) * 2);
    const v4u kb = *(const LAS v4u*)(smem + L_KP + m * KSTR + (64 * t + 32 * (ee ^ 1) + 8 * kg) * 2);
    d0 = mfma32(ka, xq[0 + t], d0);
    d1 = mfma32(ka, xq[3 + t], d1);
    d2 = mfma32(kb, xq[6 + t], d2);
    d3 = mfma32(kb, xq[9 + t], d3);
  }
  v4u vA[4], vB[4];
#pragma unroll
  for (int i = 0; i < 4; ++i) {
    vA[i] = *(const LAS v4u*)(smem + L_VP + ((bb * 4 + i) * 24 + c) * 16);
    vB[i] = *(const LAS v4u*)(smem + (a == ee ? L_VP + ((bb * 4 + i) * 24 + 16 + m) * 16 : L_ZERO));
  }
  float x[4];
#pragma unroll
  for (int i = 0; i < 4; ++i) {
    const float L = (a == ee) ? d0[i] : d1[i], Hh = (a == ee) ? d3[i] : d2[i];
    auto r = __builtin_amdgcn_permlane32_swap(__float_as_uint(L), __float_as_uint(Hh), false, false);
    const float o8 = __uint_as_float(__builtin_amdgcn_mov_dpp((int)r[1], 0x128, 0xf, 0xf, true));
    const float sc = __uint_as_float(r[0]) + o8;
    x[i] = rbf(rbf(sc) * 0.07216878364870322f);
  }
  float mx = fmaxf(fmaxf(x[0], x[1]), fmaxf(x[2], x[3]));
  {
    auto r = __builtin_amdgcn_permlane16_swap(__float_as_uint(mx), __float_as_uint(mx), false, false);
    mx = fmaxf(__uint_as_float(r[0]), __uint_as_float(r[1]));
  }
  float p[4];
#pragma unroll
  for (int i = 0; i < 4; ++i) p[i] = __builtin_amdgcn_exp2f((x[i] - mx) * 1.4426950408889634f);
  float sm = (p[0] + p[1]) + (p[2] + p[3]);
  {
    auto r = __builtin_amdgcn_permlane16_swap(__float_as_uint(sm), __float_as_uint(sm), false, false);
    sm = __uint_as_float(r[0]) + __uint_as_float(r[1]);
  }
  const float inv = __builtin_amdgcn_rcpf(sm);
  const v2u P = {pk(p[0] * inv, p[1] * inv), pk(p[2] * inv, p[3] * inv)};
  const v2u Plo = {a ? 0u : P[0], a ? 0u : P[1]}, Phi = {a ? P[0] : 0u, a ? P[1] : 0u};
  const unsigned offOA = 4 * kg * ROWB + c * 16;
  const unsigned offOB = (16 * ee + 4 * kg) * ROWB + 256 + m * 16;
#define PVTILE(PA, VB, VOFF, SOFF) do { f32x4 o_[8]; \
    _Pragma("unroll") for (int jj = 0; jj < 8; ++jj) { const v2u bv = {VB[jj >> 1][(jj & 1) * 2], VB[jj >> 1][(jj & 1) * 2 + 1]}; o_[jj] = mfma16(PA, bv, z4); } \
    _Pragma("unroll") for (int i = 0; i < 4; ++i) { \
      const v4u u = {pk(o_[0][i], o_[1][i]), pk(o_[2][i], o_[3][i]), pk(o_[4][i], o_[5][i]), pk(o_[6][i], o_[7][i])}; \
      st16<ST_AUX>(u, rO, (VOFF) + i * ROWB, (SOFF)); } } while (0)
  PVTILE(Plo, vA, offOA, sQ);
  PVTILE(P, vB, offOB, sQ);
  PVTILE(Phi, vA, offOA, sQ + 16 * ROWB);
}

extern "C" void kernel_launch(void* const* d_in, const int* in_sizes, int n_in, void* d_out, int out_size,
                              void* d_ws, size_t ws_size, hipStream_t stream) {
  (void)in_sizes; (void)n_in; (void)out_size; (void)ws_size;
  hipFuncSetAttribute((const void*)linformer_fused, hipFuncAttributeMaxDynamicSharedMemorySize, LDS_BYTES);
  linformer_fused<<<256, 512, LDS_BYTES, stream>>>((const char*)d_in[0], (const char*)d_in[1], (const char*)d_in[2],
                                                   (const char*)d_in[3], (const char*)d_in[4], (char*)d_out, (char*)d_ws);
}
```

```cpp
#include <hip/hip_runtime.h>
#include <hip/hip_bf16.h>

typedef unsigned v4u __attribute__((ext_vector_type(4)));
typedef unsigned v2u __attribute__((ext_vector_type(2)));
typedef float f32x4 __attribute__((ext_vector_type(4)));
typedef float f32x2 __attribute__((ext_vector_type(2)));
typedef __bf16 bf8 __attribute__((ext_vector_type(8)));
typedef __bf16 bf2 __attribute__((ext_vector_type(2)));
typedef short s4 __attribute__((ext_vector_type(4)));
#define GAS __attribute__((address_space(1)))
#define LAS __attribute__((address_space(3)))
typedef GAS unsigned long long gu64;

#define QLATE 2
#define QLATE2 2
#define QMID 2
#define KVHINT 4
#define HINTN 3
#define QTIMER 760
#define PDELAY 100
#define PDELAY2 200
#define PSLEEP 2

constexpr int NH = 32, NSEQ = 2048, ROWB = 384;
constexpr unsigned HEADB = NSEQ * ROWB;
constexpr int AUX_NT = 2, AUX_SC1 = 16;
constexpr unsigned WS_CTL = 0;
constexpr unsigned WS_REC = 8192;
constexpr unsigned WS_END = WS_REC + NH * 6 * 4 * 512;
constexpr int L_RED = 0;
constexpr int KSTR = 400;
constexpr int L_KP = 32768;
constexpr int L_VP = L_KP + 8 * KSTR;
constexpr int L_ZERO = L_VP + 2 * 4 * 24 * 16;
constexpr int L_CNT = L_ZERO + 16;
constexpr int L_END = L_CNT + 48;
constexpr int LDS_BYTES = 84 * 1024;
static_assert(L_END <= LDS_BYTES, "lds");
enum { C_READY = 0, C_KVISSUED = 1, C_PARTIAL = 2, C_EPOCH = 3, C_POLLED = 4, C_STORED = 5, C_GO = 6 };

template <int AUX> __device__ __forceinline__ v4u ld16(__amdgpu_buffer_rsrc_t r, unsigned voff, unsigned soff) {
  return __builtin_amdgcn_raw_buffer_load_b128(r, voff, soff, AUX);
}
template <int AUX> __device__ __forceinline__ void st16(v4u x, __amdgpu_buffer_rsrc_t r, unsigned voff) {
  __builtin_amdgcn_raw_buffer_store_b128(x, r, voff, 0, AUX);
}
__device__ __forceinline__ __amdgpu_buffer_rsrc_t rsrc(const char* p, unsigned bytes) {
  return __builtin_amdgcn_make_buffer_rsrc((void*)p, 0, bytes, 0x00020000);
}
__device__ __forceinline__ f32x4 mfma32(v4u a, v4u b, f32x4 c) {
  return __builtin_amdgcn_mfma_f32_16x16x32_bf16(__builtin_bit_cast(bf8, a), __builtin_bit_cast(bf8, b), c, 0, 0, 0);
}
__device__ __forceinline__ f32x4 mfma16(v2u a, v2u b, f32x4 c) {
  return __builtin_amdgcn_mfma_f32_16x16x16bf16_1k(__builtin_bit_cast(s4, a), __builtin_bit_cast(s4, b), c, 0, 0, 0);
}
__device__ __forceinline__ unsigned pk(float lo, float hi) {
  f32x2 v = {lo, hi};
  bf2 r = __builtin_convertvector(v, bf2);
  return __builtin_bit_cast(unsigned, r);
}
__device__ __forceinline__ float rbf(float x) { return __uint_as_float(pk(x, x) << 16); }
#define SB() __builtin_amdgcn_sched_barrier(0)
#define MEMFENCE() asm volatile("" ::: "memory")
__device__ __forceinline__ void lds_inc(LAS unsigned* p) { __hip_atomic_fetch_add(p, 1u, __ATOMIC_RELAXED, __HIP_MEMORY_SCOPE_WORKGROUP); }
__device__ __forceinline__ void lds_wait(LAS unsigned* p, unsigned want) {
#pragma nounroll
  for (unsigned spins = 0; spins < (1u << 22); ++spins) {
    if (__builtin_amdgcn_readfirstlane(__hip_atomic_load(p, __ATOMIC_RELAXED, __HIP_MEMORY_SCOPE_WORKGROUP)) >= want) break;
    __builtin_amdgcn_s_sleep(1);
  }
  MEMFENCE();
}
__device__ __forceinline__ v4u colpair(const v4u (&x)[8], int jj) {
  const unsigned sel = (jj & 1) ? 0x07060302u : 0x05040100u;
  v4u o;
  o[0] = __builtin_amdgcn_perm(x[1][jj >> 1], x[0][jj >> 1], sel);
  o[1] = __builtin_amdgcn_perm(x[3][jj >> 1], x[2][jj >> 1], sel);
  o[2] = __builtin_amdgcn_perm(x[5][jj >> 1], x[4][jj >> 1], sel);
  o[3] = __builtin_amdgcn_perm(x[7][jj >> 1], x[6][jj >> 1], sel);
  return o;
}

__global__ void __launch_bounds__(768, 3)
linformer_fused(const char* __restrict__ q, const char* __restrict__ k, const char* __restrict__ v,
                const char* __restrict__ ep, const char* __restrict__ fp, char* __restrict__ out, char* ws) {
  extern __shared__ __attribute__((aligned(16))) char smem[];
  const int tid = threadIdx.x, l = tid & 63;
  const int w = __builtin_amdgcn_readfirstlane(tid >> 6);
  const int blk = blockIdx.x;
  const int h = (blk & 7) * 4 + ((blk >> 3) & 3), j = blk >> 5;
  const bool producer = j < 6;
  const int a = l >> 5, bb = (l >> 4) & 1, ee = (l >> 3) & 1, m = l & 7, kg = l >> 4, c = l & 15;
  LAS unsigned* cnt = (LAS unsigned*)(smem + L_CNT);
  asm volatile("" :: "s"(q), "s"(k), "s"(v), "s"(ep), "s"(fp), "s"(out), "s"(ws));
  const f32x4 z4 = {0.f, 0.f, 0.f, 0.f};

  if (w >= 8) {
    const int cw = w - 8;
    const unsigned long long tstart = __builtin_amdgcn_s_memrealtime();
    if (tid < 512 + 4) *(LAS v4u*)(smem + L_ZERO + (tid - 512) * 16) = (v4u){0u, 0u, 0u, 0u};
    asm volatile("s_waitcnt lgkmcnt(0)" ::: "memory");
    __builtin_amdgcn_s_barrier();
    MEMFENCE();
    __builtin_amdgcn_s_setprio(3);
    const __amdgpu_buffer_rsrc_t rW = rsrc(ws, WS_END);
    gu64* ctl = (gu64*)(ws + WS_CTL + h * 256);
    const unsigned long long next = __hip_atomic_load(ctl, __ATOMIC_RELAXED, __HIP_MEMORY_SCOPE_AGENT) + 1ull;
    const unsigned long long tag = (next * 0x9E3779B97F4A7C15ull) | 1ull;
    const unsigned T0 = __builtin_amdgcn_readfirstlane((unsigned)tag), T1 = __builtin_amdgcn_readfirstlane((unsigned)(tag >> 32));
    asm volatile("" :: "s"(T0), "s"(T1) : "memory");
    if (l == 0) lds_inc(cnt + C_EPOCH);
    if (cw == 0) {
      lds_wait(cnt + C_EPOCH, 4);
      if (l == 0) __hip_atomic_store(ctl + 16 + j, tag, __ATOMIC_RELAXED, __HIP_MEMORY_SCOPE_AGENT);
    }
    const unsigned vo = a * 2048 + (l & 31) * 16;
    const unsigned sb = WS_REC + h * 6 * 2048 + cw * 512;
    if (producer) {
      if (cw == 1) {
        lds_wait(cnt + C_KVISSUED, KVHINT);
        if (l == 0) __hip_atomic_store(ctl + 24 + j, tag, __ATOMIC_RELAXED, __HIP_MEMORY_SCOPE_AGENT);
      }
      const unsigned so = bb == 0 ? WS_REC + ((h * 6 + j) * 4 + cw) * 512 + (a * 16 + (l & 15)) * 16u : 0x80000000u;
      lds_wait(cnt + C_PARTIAL, 8);
      const LAS f32x4* red = (const LAS f32x4*)(smem + L_RED) + cw * 64 + l;
      f32x4 t[8];
#pragma unroll
      for (int i = 0; i < 8; ++i) t[i] = red[i * 256];
      f32x4 s = ((t[0] + t[1]) + (t[2] + t[3])) + ((t[4] + t[5]) + (t[6] + t[7]));
#pragma unroll
      for (int i = 0; i < 4; ++i) {
        auto r = __builtin_amdgcn_permlane16_swap(__float_as_uint(s[i]), __float_as_uint(s[i]), false, false);
        s[i] = __uint_as_float(r[0]) + __uint_as_float(r[1]);
      }
      const unsigned p0 = pk(s[0], s[1]), p1 = pk(s[2], s[3]);
      st16<AUX_SC1>((v4u){p0, p1, p0 ^ T0, p1 ^ T1}, rW, so);
      SB();
      if (l == 0) lds_inc(cnt + C_STORED);
    } else if (cw == 1) {
#pragma nounroll
      for (unsigned spins = 0; spins < (1u << 16); ++spins) {
        if (__builtin_amdgcn_s_memrealtime() - tstart >= (unsigned long long)QTIMER) break;
        const unsigned long long x = __hip_atomic_load(ctl + 24 + (l < 6 ? l : 0), __ATOMIC_RELAXED, __HIP_MEMORY_SCOPE_AGENT);
        if (__builtin_popcountll(__ballot(l < 6 && x == tag)) >= HINTN) break;
        __builtin_amdgcn_s_sleep(4);
      }
      if (l == 0) lds_inc(cnt + C_GO);
    } else lds_wait(cnt + C_GO, 1);
    v4u x0, x1, x2;
    {
      const unsigned long long tn = __builtin_amdgcn_s_memrealtime() + (producer ? PDELAY : PDELAY2);
      while ((long long)(__builtin_amdgcn_s_memrealtime() - tn) < 0) __builtin_amdgcn_s_sleep(1);
      unsigned spins = 0;
#pragma nounroll
      for (; spins < (1u << 16); ++spins) {
        MEMFENCE();
        x0 = ld16<AUX_SC1>(rW, vo, sb); x1 = ld16<AUX_SC1>(rW, vo, sb + 4096); x2 = ld16<AUX_SC1>(rW, vo, sb + 8192);
        SB();
        if (spins == 0 && l == 0) lds_inc(cnt + C_POLLED);
        SB();
        const bool ok = ((x0[0] ^ x0[2]) == T0) & ((x0[1] ^ x0[3]) == T1) & ((x1[0] ^ x1[2]) == T0) & ((x1[1] ^ x1[3]) == T1) &
                        ((x2[0] ^ x2[2]) == T0) & ((x2[1] ^ x2[3]) == T1);
        if (__all(ok)) break;
        __builtin_amdgcn_s_sleep(PSLEEP);
      }
    }
    {
      const int jl = (l >> 4) & 1, pb = (l >> 3) & 1, sl = 2 * cw + jl;
      LAS unsigned short* kp = (LAS unsigned short*)(smem + L_KP + (4 * pb) * KSTR + (8 * m + sl) * 2);
      LAS char* vp = (LAS char*)(smem + L_VP + ((pb * 4 + cw) * 24 + m) * 16 + jl * 8);
      { LAS unsigned short* d = kp + 64 * a; d[0] = (unsigned short)x0[0]; d[KSTR / 2] = (unsigned short)(x0[0] >> 16); d[KSTR] = (unsigned short)x0[1]; d[3 * KSTR / 2] = (unsigned short)(x0[1] >> 16); }
      if (a == 0) { LAS unsigned short* d = kp + 128; d[0] = (unsigned short)x1[0]; d[KSTR / 2] = (unsigned short)(x1[0] >> 16); d[KSTR] = (unsigned short)x1[1]; d[3 * KSTR / 2] = (unsigned short)(x1[1] >> 16); }
      else *(LAS v2u*)vp = (v2u){x1[0], x1[1]};
      *(LAS v2u*)(vp + (8 + 8 * a) * 16) = (v2u){x2[0], x2[1]};
    }
    asm volatile("s_waitcnt lgkmcnt(0)" ::: "memory");
    if (l == 0) lds_inc(cnt + C_READY);
    if (j == 0 && cw == 0) {
#pragma nounroll
      for (unsigned spins = 0; spins < (1u << 16); ++spins) {
        const unsigned long long x = __hip_atomic_load(ctl + 16 + (l & 7), __ATOMIC_RELAXED, __HIP_MEMORY_SCOPE_AGENT);
        if (__all(x == tag)) break;
        __builtin_amdgcn_s_sleep(4);
      }
      if (l == 0) __hip_atomic_store(ctl, next, __ATOMIC_RELAXED, __HIP_MEMORY_SCOPE_AGENT);
    }
    return;
  }

  if (producer) {
    const int kv = j >= 3, t = j - 3 * kv;
    const __amdgpu_buffer_rsrc_t rKV = rsrc((kv ? v : k) + (size_t)h * HEADB, HEADB);
    const __amdgpu_buffer_rsrc_t rE = rsrc(kv ? fp : ep, 8 * NSEQ * 2);
    const unsigned n0 = w * 256;
    const unsigned offE = (m * NSEQ + 32 * ee + 8 * kg) * 2;
    v4u eA[4];
#pragma unroll
    for (int g = 0; g < 4; ++g) eA[g] = ld16<0>(rE, offE + g * 128, n0 * 2);
    SB();
    const unsigned offB = (32 * ee + 8 * kg) * ROWB + m * 16;
    const unsigned sKV = n0 * ROWB + t * 128;
    v4u xa[8], xb[8], xc[8];
#pragma unroll
    for (int i = 0; i < 8; ++i) xa[i] = ld16<AUX_NT>(rKV, offB + i * ROWB, sKV);
#pragma unroll
    for (int i = 0; i < 8; ++i) xb[i] = ld16<AUX_NT>(rKV, offB + i * ROWB, sKV + 64 * ROWB);
#pragma unroll
    for (int i = 0; i < 8; ++i) xc[i] = ld16<AUX_NT>(rKV, offB + i * ROWB, sKV + 128 * ROWB);
    SB();
    __builtin_amdgcn_s_barrier();
    MEMFENCE();
    SB();
    f32x4 acc[8];
#pragma unroll
    for (int jj = 0; jj < 8; ++jj) acc[jj] = mfma32(eA[0], colpair(xa, jj), z4);
    SB();
#pragma unroll
    for (int i = 0; i < 8; ++i) xa[i] = ld16<AUX_NT>(rKV, offB + i * ROWB, sKV + 192 * ROWB);
    SB();
    if (l == 0) lds_inc(cnt + C_KVISSUED);
    SB();
#pragma unroll
    for (int jj = 0; jj < 8; ++jj) acc[jj] = mfma32(eA[1], colpair(xb, jj), acc[jj]);
#pragma unroll
    for (int jj = 0; jj < 8; ++jj) acc[jj] = mfma32(eA[2], colpair(xc, jj), acc[jj]);
#pragma unroll
    for (int jj = 0; jj < 8; ++jj) acc[jj] = mfma32(eA[3], colpair(xa, jj), acc[jj]);
    if (a == ee) {
      LAS f32x4* red = (LAS f32x4*)(smem + L_RED) + w * 256 + a * 16 + bb * 8 + m;
#pragma unroll
      for (int jj = 0; jj < 8; ++jj) red[jj * 32] = acc[jj];
    }
    asm volatile("s_waitcnt lgkmcnt(0)" ::: "memory");
    if (l == 0) lds_inc(cnt + C_PARTIAL);
  } else {
    __builtin_amdgcn_s_barrier();
    MEMFENCE();
  }

  const unsigned q0 = j * 256 + w * 32;
  const unsigned sQ0 = q0 * ROWB, sQ1 = sQ0 + 8 * ROWB, sQ2 = sQ0 + 24 * ROWB, sQ3 = sQ0 + 16 * ROWB;
  unsigned offQ0 = m * ROWB + (4 * ee + kg) * 16, offQ1 = m * ROWB + (4 * (ee ^ 1) + kg) * 16;
  const char* qh = q + (size_t)h * HEADB;
  const char* oh = out + (size_t)h * HEADB;
  unsigned lka = L_KP + m * KSTR + (32 * ee + 8 * kg) * 2, lkb = L_KP + m * KSTR + (32 * (ee ^ 1) + 8 * kg) * 2;
  unsigned lva = L_VP + (bb * 4 * 24 + c) * 16, lvb = a == ee ? L_VP + (bb * 4 * 24 + 16 + m) * 16 : L_ZERO;
  unsigned lvs = a == ee ? 24 * 16 : 0;
  unsigned oA = sQ0 + 4 * kg * ROWB + c * 16, oB = sQ0 + (16 * ee + 4 * kg) * ROWB + 256 + m * 16;
  asm volatile("" : "+v"(offQ0), "+v"(offQ1), "+v"(lka), "+v"(lkb), "+v"(lva), "+v"(lvb), "+v"(lvs), "+v"(oA), "+v"(oB)
               : "s"(sQ0), "s"(sQ1), "s"(sQ2), "s"(sQ3), "s"(qh), "s"(oh));
  const __amdgpu_buffer_rsrc_t rQ = rsrc(qh, HEADB), rO = rsrc(oh, HEADB);
  if (w >= 8 - (producer ? QLATE : QLATE2)) lds_wait(cnt + C_POLLED, 4);
  else if (!producer) lds_wait(cnt + C_GO, 1);
  else if (w >= 8 - QLATE - QMID) lds_wait(cnt + C_STORED, 4);
  else lds_wait(cnt + C_KVISSUED, 8);
  SB();
  v4u xq[12];
#pragma unroll
  for (int t = 0; t < 3; ++t) xq[t] = ld16<AUX_NT>(rQ, offQ0 + t * 128, sQ0);
#pragma unroll
  for (int t = 0; t < 3; ++t) xq[3 + t] = ld16<AUX_NT>(rQ, offQ1 + t * 128, sQ1);
#pragma unroll
  for (int t = 0; t < 3; ++t) xq[6 + t] = ld16<AUX_NT>(rQ, offQ0 + t * 128, sQ2);
#pragma unroll
  for (int t = 0; t < 3; ++t) xq[9 + t] = ld16<AUX_NT>(rQ, offQ1 + t * 128, sQ3);
  SB();
  lds_wait(cnt + C_READY, 4);
  SB();

  f32x4 d0 = z4, d1 = z4, d2 = z4, d3 = z4;
#pragma unroll
  for (int t = 0; t < 3; ++t) {
    const v4u ka = *(const LAS v4u*)(smem + lka + 128 * t);
    const v4u kb = *(const LAS v4u*)(smem + lkb + 128 * t);
    d0 = mfma32(ka, xq[0 + t], d0);
    d1 = mfma32(ka, xq[3 + t], d1);
    d2 = mfma32(kb, xq[6 + t], d2);
    d3 = mfma32(kb, xq[9 + t], d3);
  }
  v4u vA[4], vB[4];
#pragma unroll
  for (int i = 0; i < 4; ++i) {
    vA[i] = *(const LAS v4u*)(smem + lva + i * 24 * 16);
    vB[i] = *(const LAS v4u*)(smem + lvb + i * lvs);
  }
  float x[4];
#pragma unroll
  for (int i = 0; i < 4; ++i) {
    const float L = (a == ee) ? d0[i] : d1[i], Hh = (a == ee) ? d3[i] : d2[i];
    auto r = __builtin_amdgcn_permlane32_swap(__float_as_uint(L), __float_as_uint(Hh), false, false);
    const float o8 = __uint_as_float(__builtin_amdgcn_mov_dpp((int)r[1], 0x128, 0xf, 0xf, true));
    x[i] = rbf(rbf(__uint_as_float(r[0]) + o8) * 0.07216878364870322f);
  }
  float mx = fmaxf(fmaxf(x[0], x[1]), fmaxf(x[2], x[3]));
  {
    auto r = __builtin_amdgcn_permlane16_swap(__float_as_uint(mx), __float_as_uint(mx), false, false);
    mx = fmaxf(__uint_as_float(r[0]), __uint_as_float(r[1]));
  }
  float p[4];
#pragma unroll
  for (int i = 0; i < 4; ++i) p[i] = __builtin_amdgcn_exp2f((x[i] - mx) * 1.4426950408889634f);
  float sm = (p[0] + p[1]) + (p[2] + p[3]);
  {
    auto r = __builtin_amdgcn_permlane16_swap(__float_as_uint(sm), __float_as_uint(sm), false, false);
    sm = __uint_as_float(r[0]) + __uint_as_float(r[1]);
  }
  const float inv = __builtin_amdgcn_rcpf(sm);
  const v2u P = {pk(p[0] * inv, p[1] * inv), pk(p[2] * inv, p[3] * inv)};
  const v2u Plo = {a ? 0u : P[0], a ? 0u : P[1]}, Phi = {a ? P[0] : 0u, a ? P[1] : 0u};
#define PVTILE(PA, VB, VOFF) do { f32x4 o_[8]; \
    _Pragma("unroll") for (int jj = 0; jj < 8; ++jj) { const v2u bv = {VB[jj >> 1][(jj & 1) * 2], VB[jj >> 1][(jj & 1) * 2 + 1]}; o_[jj] = mfma16(PA, bv, z4); } \
    _Pragma("unroll") for (int i = 0; i < 4; ++i) { \
      const v4u u = {pk(o_[0][i], o_[1][i]), pk(o_[2][i], o_[3][i]), pk(o_[4][i], o_[5][i]), pk(o_[6][i], o_[7][i])}; \
      st16<AUX_NT>(u, rO, (VOFF) + i * ROWB); } } while (0)
  PVTILE(Plo, vA, oA);
  PVTILE(P, vB, oB);
  PVTILE(Phi, vA, oA + 16 * ROWB);
}

extern "C" void kernel_launch(void* const* d_in, const int* in_sizes, int n_in, void* d_out, int out_size,
                              void* d_ws, size_t ws_size, hipStream_t stream) {
  (void)in_sizes; (void)n_in; (void)out_size; (void)ws_size;
  (void)hipFuncSetAttribute((const void*)linformer_fused, hipFuncAttributeMaxDynamicSharedMemorySize, LDS_BYTES);
  linformer_fused<<<256, 768, LDS_BYTES, stream>>>((const char*)d_in[0], (const char*)d_in[1], (const char*)d_in[2],
                                                   (const char*)d_in[3], (const char*)d_in[4], (char*)d_out, (char*)d_ws);
}
```
